```python
import torch

def _softmax(x, axis=-1):
    return torch.softmax(x, dim=axis)
def _gelu(x):
    return torch.nn.functional.gelu(x, approximate="tanh")
def _silu(x):
    return torch.nn.functional.silu(x)
def _sigmoid(x):
    return torch.sigmoid(x)
def _layernorm(x, axis=-1, eps=1e-5):
    m = x.mean(dim=axis, keepdim=True)
    v = x.var(dim=axis, keepdim=True, unbiased=False)
    return (x - m) / torch.sqrt(v + eps)
def _scatter_add(tgt, idx, src, axis=0):
    return tgt.clone().index_add_(axis, idx, src)
def _segment_sum(src, seg, n):
    out = torch.zeros((n,) + src.shape[1:], dtype=src.dtype)
    return out.index_add_(0, seg, src)
def _pad(x, pw):
    flat = tuple(v for pair in reversed(pw) for v in pair)
    return torch.nn.functional.pad(x, flat)

kernel_name = 'parallel_sum__gqa_block_cached__glu_gated_swiglu__decode'

def setup_inputs():

    _w = (lambda a: (lambda t: t.to(torch.bfloat16) if t.is_floating_point() else t)(torch.as_tensor(a)))
    def _A_setup():
        B, Sq, D = 1, 1, 512
        Hq, Hkv, Sk, Dh = 32, 8, 65536, 128
        assert Hq % Hkv == 0, (Hq, Hkv)
        return {
            "x": _w((lambda *s: torch.randn(*s, dtype=torch.bfloat16) * 0.05)(B, Sq, D)),
            "k_cache": _w((lambda *s: torch.randn(*s, dtype=torch.bfloat16) * 3.2)(B, Hkv, Sk, Dh)),
            "v_cache": _w((lambda *s: torch.randn(*s, dtype=torch.bfloat16) * 1.6)(B, Hkv, Sk, Dh)),
            "wq": _w((lambda *s: torch.randn(*s, dtype=torch.bfloat16) * 3.2)(D, Hq * Dh) * (D ** -0.5)),
            "wo": _w((lambda *s: torch.randn(*s, dtype=torch.bfloat16) * 1.6)(Hq * Dh, D) * ((Hq * Dh) ** -0.5)),
        }
    _A_in = _A_setup()

    def _B_setup():
        B, S, D, F = 1, 1, 512, 1792
        return {
            "x": _w((lambda *s: torch.randn(*s, dtype=torch.bfloat16) * 0.05)(B, S, D)),
            "w1": _w((lambda *s: torch.randn(*s, dtype=torch.bfloat16) * 1.6)(D, F) * (2.0 / D) ** 0.5),
            "w3": _w((lambda *s: torch.randn(*s, dtype=torch.bfloat16) * 1.6)(D, F) * (2.0 / D) ** 0.5),
            "w2": _w((lambda *s: torch.randn(*s, dtype=torch.bfloat16) * 1.6)(F, D) * (2.0 / F) ** 0.5),
        }
    _B_in = _B_setup()

    return {
        "A_x": _A_in["x"],
        "A_k_cache": _A_in["k_cache"],
        "A_v_cache": _A_in["v_cache"],
        "A_wq": _A_in["wq"],
        "A_wo": _A_in["wo"],
        "B_x": _B_in["x"],
        "B_w1": _B_in["w1"],
        "B_w3": _B_in["w3"],
        "B_w2": _B_in["w2"],
    }

def reference(A_x, A_k_cache, A_v_cache, A_wq, A_wo, B_x, B_w1, B_w3, B_w2):
    def _A_ref(x, k_cache, v_cache, wq, wo):
        B, Sq, D = x.shape
        Hq, Dh = 32, 128
        Hkv = k_cache.shape[1]
        rep = Hq // Hkv
        q = (lambda x, *d: x.permute(*d))((x @ wq).reshape(B, Sq, Hq, Dh), 0, 2, 1, 3)
        k = (lambda x, n, axis: torch.repeat_interleave(x, n, dim=axis))(k_cache, rep, axis=1)
        v = (lambda x, n, axis: torch.repeat_interleave(x, n, dim=axis))(v_cache, rep, axis=1)
        scores = torch.einsum("bhqd,bhkd->bhqk", q, k) * (Dh ** -0.5)
        p = _softmax(scores, axis=-1)
        o = torch.einsum("bhqk,bhkd->bhqd", p, v)
        return (lambda x, *d: x.permute(*d))(o, 0, 2, 1, 3).reshape(B, Sq, Hq * Dh) @ wo

    def _B_ref(x, w1, w3, w2):
        gate = torch.einsum("bsd,df->bsf", x, w1)
        up = torch.einsum("bsd,df->bsf", x, w3)
        h = _silu(gate) * up
        out = torch.einsum("bsf,fd->bsd", h, w2)
        return out

    x = A_x
    xn = _layernorm(x, axis=-1)
    return x + _A_ref(xn, A_k_cache, A_v_cache, A_wq, A_wo) + _B_ref(xn, B_w1, B_w3, B_w2)
```

```cpp
#include <hip/hip_runtime.h>
#include <hip/hip_bf16.h>
#include <stdint.h>

typedef unsigned int u32;
typedef unsigned long long u64;
typedef unsigned short u16;
typedef __attribute__((ext_vector_type(4))) unsigned u32x4;
typedef __attribute__((ext_vector_type(4))) float f32x4;
typedef __attribute__((ext_vector_type(8))) __bf16 bf16x8;
typedef __attribute__((ext_vector_type(4))) short s16x4;
typedef __attribute__((ext_vector_type(2))) unsigned u32x2;
typedef __attribute__((ext_vector_type(2))) float f32x2;
typedef __attribute__((ext_vector_type(2))) __bf16 bf16x2;
#define GAS __attribute__((address_space(1)))
typedef GAS u64 gu64;
typedef GAS u32 gu32;
#define RLX_AGENT __ATOMIC_RELAXED, __HIP_MEMORY_SCOPE_AGENT

#define PRE 2
#define TIMING 0

namespace {
constexpr int NWG = 256, NSW = 8, NTHR = (NSW + 1) * 64;
constexpr int DM = 512, SK = 65536, DH = 128, FF = 1792;
constexpr int SPLITS = 32, KEYS_WG = SK / SPLITS;
constexpr int T = 4;
constexpr int NTILE = KEYS_WG / (NSW * 4 * T);
constexpr int NFWG = FF / 8;
constexpr float SCALE = 0.08838834764831845f;
constexpr float LOG2E = 1.4426950408889634f;
constexpr u32 SPIN_CAP = 1u << 22;

constexpr size_t WS_EPOCH = 0;
constexpr size_t WS_HOP0 = 4096;
constexpr size_t WS_HOP1M = WS_HOP0 + 8 * 256 * 8;
constexpr size_t WS_HOP1A = 65536;
constexpr size_t WS_HOPF = WS_HOP1A + (size_t)8 * 32 * 32 * 16 * 8;
constexpr size_t WS_HOP2 = WS_HOPF + (size_t)256 * 256 * 2 * 8;
constexpr size_t WS_DBG = WS_HOP2 + (size_t)256 * 256 * 2 * 8;

__device__ __forceinline__ float bf2f(u32 h) { return __builtin_bit_cast(float, h << 16); }
__device__ __forceinline__ float bflo(u32 w) { return __builtin_bit_cast(float, w << 16); }
__device__ __forceinline__ float bfhi(u32 w) { return __builtin_bit_cast(float, w & 0xffff0000u); }
__device__ __forceinline__ u32 cvtpk(float lo, float hi) {
  const f32x2 v = {lo, hi};
  return __builtin_bit_cast(u32, __builtin_convertvector(v, bf16x2));
}
__device__ __forceinline__ float bfr(float x) { return bflo(cvtpk(x, x)); }
template <int CTRL> __device__ __forceinline__ float dpp(float x) {
  return __builtin_bit_cast(float, __builtin_amdgcn_mov_dpp(__builtin_bit_cast(int, x), CTRL, 0xf, 0xf, true));
}
__device__ __forceinline__ float xrow_max(float x) {
  auto s = __builtin_amdgcn_permlane16_swap(__float_as_uint(x), __float_as_uint(x), false, false);
  x = fmaxf(__uint_as_float(s[0]), __uint_as_float(s[1]));
  auto t = __builtin_amdgcn_permlane32_swap(__float_as_uint(x), __float_as_uint(x), false, false);
  return fmaxf(__uint_as_float(t[0]), __uint_as_float(t[1]));
}
__device__ __forceinline__ float xrow_sum(float x) {
  auto s = __builtin_amdgcn_permlane16_swap(__float_as_uint(x), __float_as_uint(x), false, false);
  x = __uint_as_float(s[0]) + __uint_as_float(s[1]);
  auto t = __builtin_amdgcn_permlane32_swap(__float_as_uint(x), __float_as_uint(x), false, false);
  return __uint_as_float(t[0]) + __uint_as_float(t[1]);
}
__device__ __forceinline__ float sum_par(float x) {
  x += dpp<0x4E>(x);
  x += dpp<0x124>(x);
  x += dpp<0x128>(x);
  return xrow_sum(x);
}
__device__ __forceinline__ float sum_all(float x) { x += dpp<0xB1>(x); return sum_par(x); }
__device__ __forceinline__ float max_all(float x) {
  x = fmaxf(x, dpp<0xB1>(x)); x = fmaxf(x, dpp<0x4E>(x)); x = fmaxf(x, dpp<0x124>(x)); x = fmaxf(x, dpp<0x128>(x));
  return xrow_max(x);
}
__device__ __forceinline__ u32x4 ldnt(const void* p) { return __builtin_nontemporal_load((const u32x4*)p); }
__device__ __forceinline__ u32 ldnt16(const u16* p) { return (u32)__builtin_nontemporal_load(p); }
__device__ __forceinline__ void put(gu64* g, u32 tag, u32 v) { __hip_atomic_store(g, ((u64)tag << 32) | v, RLX_AGENT); }
__device__ __forceinline__ void putf(gu64* g, u32 tag, float v) { put(g, tag, __float_as_uint(v)); }
__device__ __forceinline__ u64 get(const gu64* g) { return __hip_atomic_load(g, RLX_AGENT); }
__device__ __forceinline__ void wg_barrier() {
  asm volatile("s_waitcnt lgkmcnt(0)" ::: "memory");
  __builtin_amdgcn_s_barrier();
  asm volatile("" ::: "memory");
}
#define FENCE() do { asm volatile("" ::: "memory"); __builtin_amdgcn_sched_barrier(0); } while (0)
#define STAMP(i) do { } while (0)

__global__ void __launch_bounds__(NTHR, 3)
fused_decode(const u16* __restrict__ x, const char* __restrict__ kc, const char* __restrict__ vc,
             const u16* __restrict__ wq, const u16* __restrict__ wo, const u16* __restrict__ w1,
             const u16* __restrict__ w3, const u16* __restrict__ w2, u32* __restrict__ out, char* ws) {
  __shared__ __attribute__((aligned(16))) float sm_o[NSW][4][DH];
  __shared__ __attribute__((aligned(16))) float sm_ml[NSW][4][2];
  __shared__ __attribute__((aligned(16))) float sm_red[NSW][8];
  __shared__ __attribute__((aligned(16))) u32x4 sm_q[64];
  __shared__ __attribute__((aligned(16))) float sm_ov[16];
  __shared__ __attribute__((aligned(16))) u32 sm_tag[4];

  const int tid = threadIdx.x, lane = tid & 63;
  const int wave = __builtin_amdgcn_readfirstlane(tid >> 6);
  const int b = blockIdx.x, g = b >> 5, s = b & 31;
  STAMP(0);
  gu64* hop0 = (gu64*)(ws + WS_HOP0);
  gu64* hop1m = (gu64*)(ws + WS_HOP1M);
  gu64* hop1a = (gu64*)(ws + WS_HOP1A);
  gu64* hopf = (gu64*)(ws + WS_HOPF);
  gu64* hop2 = (gu64*)(ws + WS_HOP2);
  const bool has_ffn = b < NFWG;
  const int cb = has_ffn ? (b & 7) * 28 + (b >> 3) : 0;
  const int sig = (s & 7) * 4 + (s >> 3);

  if (wave < NSW) {
    const u32x4 xc = ldnt(x + 8 * lane);
    const int pr = wave >> 1, u = tid & 127;
    const u16* wp = pr == 0 ? wq + g * 512 + sig * 16 : pr == 1 ? wq + g * 512 + sig * 16 + 8 : pr == 2 ? w1 + cb * 8 : w3 + cb * 8;
    const int wstr = pr < 2 ? 4096 : FF;
    u32 xr[4]; u32x4 wv[4];
#pragma unroll
    for (int i = 0; i < 4; ++i) xr[i] = ldnt16(x + u + 128 * i);
#pragma unroll
    for (int i = 0; i < 4; ++i) wv[i] = ldnt(wp + (size_t)(u + 128 * i) * wstr);
    FENCE();
    const int g4 = lane >> 4;
    const size_t wgoff = ((size_t)g * SK + (size_t)s * KEYS_WG) * 256 + (size_t)wave * (T * 1024);
    const char* kp = kc + wgoff + (((lane >> 2) & 3) * 256 + (4 * g4 + (lane & 3)) * 16);
    const char* vp = vc + wgoff + lane * 16;
    constexpr int TSTR = NSW * T * 1024;
    u32x4 ka[T], va[T], kb[T], vb[T];
#define LOADS(a, p) do { _Pragma("unroll") for (int t_ = 0; t_ < T; ++t_) a[t_] = ldnt((p) + t_ * 1024); FENCE(); } while (0)
    if (PRE >= 1) LOADS(ka, kp);
    if (PRE >= 2) LOADS(va, vp);
    if (PRE >= 3) LOADS(kb, kp + TSTR);
    if (PRE >= 4) LOADS(vb, vp + TSTR);
    u32 w2r[8], wor[16];
#pragma unroll
    for (int r = 0; r < 8; ++r) w2r[r] = ldnt16(w2 + (size_t)(cb * 8 + r) * DM + tid);
#pragma unroll
    for (int r = 0; r < 16; ++r) wor[r] = ldnt16(wo + (size_t)(b * 16 + r) * DM + tid);
    FENCE();
    float xv[8];
#pragma unroll
    for (int i = 0; i < 4; ++i) { xv[2 * i] = bflo(xc[i]); xv[2 * i + 1] = bfhi(xc[i]); }
    float sum = ((xv[0] + xv[1]) + (xv[2] + xv[3])) + ((xv[4] + xv[5]) + (xv[6] + xv[7]));
    sum = sum_all(sum);
    const float mean = sum * (1.f / DM);
    float sq = 0.f;
#pragma unroll
    for (int i = 0; i < 8; ++i) { const float d = xv[i] - mean; sq = fmaf(d, d, sq); }
    sq = sum_all(sq);
    const float mb = bfr(mean), vb_ = bfr(sq * (1.f / DM));
    const float den = bfr(sqrtf(bfr(vb_ + 1e-5f)));
    float a8[8];
#pragma unroll
    for (int j = 0; j < 8; ++j) a8[j] = 0.f;
#pragma unroll
    for (int i = 0; i < 4; ++i) {
      const float xn = bfr(bfr(bf2f(xr[i]) - mb) / den);
#pragma unroll
      for (int j = 0; j < 4; ++j) { a8[2 * j] = fmaf(xn, bflo(wv[i][j]), a8[2 * j]); a8[2 * j + 1] = fmaf(xn, bfhi(wv[i][j]), a8[2 * j + 1]); }
    }
#pragma unroll
    for (int j = 0; j < 8; ++j) a8[j] = sum_all(a8[j]);
    if (lane == 0) {
      *(f32x4*)&sm_red[wave][0] = f32x4{a8[0], a8[1], a8[2], a8[3]};
      *(f32x4*)&sm_red[wave][4] = f32x4{a8[4], a8[5], a8[6], a8[7]};
    }
    STAMP(1);
    wg_barrier();
    wg_barrier();
    STAMP(2);
    const u32x4 qf = sm_q[lane];
    const u32 tag = sm_tag[0];
    if (PRE < 1) LOADS(ka, kp);
    if (PRE < 2) LOADS(va, vp);
    if (PRE < 3) LOADS(kb, kp + TSTR);
    if (PRE < 4) LOADS(vb, vp + TSTR);
    if (has_ffn) {
      const int e = lane & 7;
      const float gt = bfr(sm_red[4][e] + sm_red[5][e]), up = bfr(sm_red[6][e] + sm_red[7][e]);
      const float sl = bfr(gt / (1.f + __expf(-gt)));
      const float hv = bfr(sl * up);
      float pf = 0.f;
#pragma unroll
      for (int r = 0; r < 8; ++r) pf = fmaf(__builtin_bit_cast(float, __builtin_amdgcn_readlane(__builtin_bit_cast(int, hv), r)), bf2f(w2r[r]), pf);
      putf(hopf + ((size_t)(tid >> 1) * 256 + b) * 2 + (tid & 1), tag, pf);
    }
    FENCE();
    kp += 2 * TSTR; vp += 2 * TSTR;
    const int jq = lane & 3;
    const u32 mask0 = jq == 0 ? 0x0000ffffu : jq == 1 ? 0xffff0000u : 0u;
    const u32 mask1 = jq == 2 ? 0x0000ffffu : jq == 3 ? 0xffff0000u : 0u;
    float m = -INFINITY, l = 0.f;
    f32x4 acc0 = {0.f, 0.f, 0.f, 0.f}, acc1 = {0.f, 0.f, 0.f, 0.f};
    float sc[T];
#define QK(kf) do { _Pragma("unroll") for (int t_ = 0; t_ < T; ++t_) { \
      const f32x4 d_ = __builtin_amdgcn_mfma_f32_16x16x32_bf16(__builtin_bit_cast(bf16x8, kf[t_]), __builtin_bit_cast(bf16x8, qf), f32x4{0.f, 0.f, 0.f, 0.f}, 0, 0, 0); \
      const float x_ = (dpp<0x00>(d_[0]) + dpp<0x55>(d_[1])) + (dpp<0xAA>(d_[2]) + dpp<0xFF>(d_[3])); \
      sc[t_] = bfr(bfr(x_) * SCALE); } \
      asm volatile("" : "+v"(sc[0]), "+v"(sc[1]), "+v"(sc[2]), "+v"(sc[3]) :: "memory"); __builtin_amdgcn_sched_barrier(0); } while (0)
#define PV(vf) do { \
      float tm_ = fmaxf(fmaxf(sc[0], sc[1]), fmaxf(sc[2], sc[3])); tm_ = xrow_max(tm_); \
      const float mn_ = fmaxf(m, tm_); const float al_ = __builtin_amdgcn_exp2f((m - mn_) * LOG2E); m = mn_; \
      l *= al_; acc0 *= al_; acc1 *= al_; \
      _Pragma("unroll") for (int t_ = 0; t_ < T; ++t_) { \
        const float p_ = __builtin_amdgcn_exp2f((sc[t_] - mn_) * LOG2E); l += p_; \
        const u32 pk_ = cvtpk(p_, p_); const u32x2 bb_ = {pk_ & mask0, pk_ & mask1}; \
        const u32x2 v0_ = {vf[t_][0], vf[t_][1]}, v1_ = {vf[t_][2], vf[t_][3]}; \
        acc0 = __builtin_amdgcn_mfma_f32_16x16x16bf16_1k(__builtin_bit_cast(s16x4, v0_), __builtin_bit_cast(s16x4, bb_), acc0, 0, 0, 0); \
        acc1 = __builtin_amdgcn_mfma_f32_16x16x16bf16_1k(__builtin_bit_cast(s16x4, v1_), __builtin_bit_cast(s16x4, bb_), acc1, 0, 0, 0); } \
      asm volatile("" : "+v"(acc0), "+v"(acc1) :: "memory"); __builtin_amdgcn_sched_barrier(0); } while (0)
    for (int i = 0; i < NTILE - 2; i += 2) {
      QK(ka); LOADS(ka, kp); PV(va); LOADS(va, vp);
      QK(kb); LOADS(kb, kp + TSTR); PV(vb); LOADS(vb, vp + TSTR);
      kp += 2 * TSTR; vp += 2 * TSTR;
    }
    QK(ka); PV(va); QK(kb); PV(vb);
    STAMP(3);
    l = xrow_sum(l);
    {
      const int h = (lane & 15) >> 2;
#pragma unroll
      for (int r = 0; r < 4; ++r) {
        sm_o[wave][h][32 * g4 + 8 * r + jq] = acc0[r];
        sm_o[wave][h][32 * g4 + 8 * r + 4 + jq] = acc1[r];
      }
      if (lane < 16 && jq == 0) { sm_ml[wave][h][0] = m; sm_ml[wave][h][1] = l; }
    }
    wg_barrier();
    {
      const int h = tid >> 7, d = tid & 127;
      float M = sm_ml[0][h][0];
#pragma unroll
      for (int w = 1; w < NSW; ++w) M = fmaxf(M, sm_ml[w][h][0]);
      float val = 0.f, L = 0.f;
#pragma unroll
      for (int w = 0; w < NSW; ++w) {
        const float e = __builtin_amdgcn_exp2f((sm_ml[w][h][0] - M) * LOG2E);
        val = fmaf(e, sm_o[w][h][d], val); L = fmaf(e, sm_ml[w][h][1], L);
      }
      putf(hop1a + (((size_t)g * 32 + (8 * h + (d >> 4))) * 32 + s) * 16 + (d & 15), tag, val);
      if (d < 2) putf(hop1m + (((size_t)g * 4 + h) * 32 + s) * 2 + d, tag, d == 0 ? M : L);
    }
    STAMP(4);
    wg_barrier();
    {
      float po = 0.f;
#pragma unroll
      for (int r = 0; r < 16; ++r) po = fmaf(sm_ov[r], bf2f(wor[r]), po);
      putf(hop2 + ((size_t)(tid >> 1) * 256 + b) * 2 + (tid & 1), tag, po);
    }
    STAMP(5);
  } else {
    const u32 tag = __hip_atomic_load((gu32*)(ws + WS_EPOCH), RLX_AGENT) + 1u;
    const u32 xo = *(const u32*)(x + 2 * b);
    if (lane == 0) sm_tag[0] = tag;
    wg_barrier();
    if (lane < 8) {
      const int w0 = (lane >> 2) * 2, e = (lane & 3) * 2;
      const float q0 = sm_red[w0][e] + sm_red[w0 + 1][e], q1 = sm_red[w0][e + 1] + sm_red[w0 + 1][e + 1];
      put(hop0 + g * 256 + sig * 8 + lane, tag, cvtpk(q0, q1));
    }
    STAMP(1);
    {
      const gu64* src = hop0 + g * 256 + ((lane & 15) >> 2) * 64 + (lane >> 4) * 16 + (lane & 3) * 4;
      u32x4 qv;
      for (u32 spin = 0; spin < SPIN_CAP; ++spin) {
        bool ok = true;
#pragma unroll
        for (int k = 0; k < 4; ++k) { const u64 v = get(src + k); qv[k] = (u32)v; ok &= (u32)(v >> 32) == tag; }
        if (__all(ok)) break;
      }
      sm_q[lane] = qv;
    }
    STAMP(2);
    wg_barrier();
    float f0 = 0.f, f1 = 0.f;
    __builtin_amdgcn_s_sleep(127);
    __builtin_amdgcn_s_sleep(127);
    {
      const gu64* src = hopf + ((size_t)b * 256 + lane) * 2;
      for (u32 spin = 0; spin < SPIN_CAP; ++spin) {
        bool ok = true; f0 = 0.f; f1 = 0.f;
#pragma unroll
        for (int i = 0; i < 4; ++i) {
          if (i < 3 || lane < NFWG - 192) {
            const u64 v0 = get(src + i * 128), v1 = get(src + i * 128 + 1);
            ok &= ((u32)(v0 >> 32) == tag) & ((u32)(v1 >> 32) == tag);
            f0 += __uint_as_float((u32)v0); f1 += __uint_as_float((u32)v1);
          }
        }
        if (__all(ok)) break;
        __builtin_amdgcn_s_sleep(32);
      }
      f0 = sum_all(f0); f1 = sum_all(f1);
    }
    STAMP(3);
    wg_barrier();
    STAMP(4);
    {
      const int sp = lane >> 1, hf = lane & 1;
      const gu64* sa = hop1a + (((size_t)g * 32 + s) * 32 + sp) * 16 + hf * 8;
      const gu64* sm = hop1m + (((size_t)g * 4 + (s >> 3)) * 32 + sp) * 2;
      float av[8], ms = 0.f, ls = 0.f;
      for (u32 spin = 0; spin < SPIN_CAP; ++spin) {
        const u64 vm = get(sm), vl = get(sm + 1);
        bool ok = ((u32)(vm >> 32) == tag) & ((u32)(vl >> 32) == tag);
        ms = __uint_as_float((u32)vm); ls = __uint_as_float((u32)vl);
#pragma unroll
        for (int k = 0; k < 8; ++k) { const u64 v = get(sa + k); av[k] = __uint_as_float((u32)v); ok &= (u32)(v >> 32) == tag; }
        if (__all(ok)) break;
      }
      STAMP(5);
      const float M = max_all(ms);
      const float w = __builtin_amdgcn_exp2f((ms - M) * LOG2E);
      const float L = sum_par(w * ls);
#pragma unroll
      for (int k = 0; k < 8; ++k) av[k] = bfr(sum_par(w * av[k]) / L);
      if (lane < 2) {
        *(f32x4*)&sm_ov[hf * 8] = f32x4{av[0], av[1], av[2], av[3]};
        *(f32x4*)&sm_ov[hf * 8 + 4] = f32x4{av[4], av[5], av[6], av[7]};
      }
    }
    wg_barrier();
    STAMP(6);
    {
      const gu64* src = hop2 + ((size_t)b * 256 + lane) * 2;
      float a0 = 0.f, a1 = 0.f;
      for (u32 spin = 0; spin < SPIN_CAP; ++spin) {
        bool ok = true; a0 = 0.f; a1 = 0.f;
#pragma unroll
        for (int i = 0; i < 4; ++i) {
          const u64 v0 = get(src + i * 128), v1 = get(src + i * 128 + 1);
          ok &= ((u32)(v0 >> 32) == tag) & ((u32)(v1 >> 32) == tag);
          a0 += __uint_as_float((u32)v0); a1 += __uint_as_float((u32)v1);
        }
        if (__all(ok)) break;
      }
      STAMP(7);
      a0 = sum_all(a0); a1 = sum_all(a1);
      if (lane == 0) {
        const float o0 = bfr(bflo(xo) + bfr(a0)) + bfr(f0), o1 = bfr(bfhi(xo) + bfr(a1)) + bfr(f1);
        out[b] = cvtpk(o0, o1);
        if (b == 0) __hip_atomic_store((gu32*)(ws + WS_EPOCH), tag, RLX_AGENT);
      }
    }
    STAMP(8);
  }
}
}

extern "C" void kernel_launch(void* const* d_in, const int* in_sizes, int n_in,
                              void* d_out, int out_size, void* d_ws, size_t ws_size,
                              hipStream_t stream) {
  (void)in_sizes; (void)n_in; (void)out_size; (void)ws_size;
  hipLaunchKernelGGL(fused_decode, dim3(NWG), dim3(NTHR), 0, stream,
                     (const u16*)d_in[0], (const char*)d_in[1], (const char*)d_in[2], (const u16*)d_in[3], (const u16*)d_in[4],
                     (const u16*)d_in[6], (const u16*)d_in[7], (const u16*)d_in[8], (u32*)d_out, (char*)d_ws);
}
```

```cpp
#include <hip/hip_runtime.h>
#include <hip/hip_bf16.h>
#include <stdint.h>

typedef unsigned int u32;
typedef unsigned long long u64;
typedef unsigned short u16;
typedef __attribute__((ext_vector_type(4))) unsigned u32x4;
typedef __attribute__((ext_vector_type(4))) float f32x4;
typedef __attribute__((ext_vector_type(8))) __bf16 bf16x8;
typedef __attribute__((ext_vector_type(4))) short s16x4;
typedef __attribute__((ext_vector_type(2))) unsigned u32x2;
typedef __attribute__((ext_vector_type(2))) float f32x2;
typedef __attribute__((ext_vector_type(2))) __bf16 bf16x2;
#define GAS __attribute__((address_space(1)))
typedef GAS u64 gu64;
typedef GAS u32 gu32;
#define RLX_AGENT __ATOMIC_RELAXED, __HIP_MEMORY_SCOPE_AGENT

#define DU 24
#define TIMING 0

namespace {
constexpr int NWG = 256, NSW = 8, NTHR = (NSW + 1) * 64;
constexpr int DM = 512, SK = 65536, DH = 128, FF = 1792;
constexpr int T = 4;
constexpr int UNITS_WG = 64;
constexpr int SU = (UNITS_WG - DU) / NSW;
constexpr int POOL = 32 * DU;
static_assert(SU >= 2 && SU * NSW + DU == UNITS_WG, "unit split");
constexpr int NFP = 56;
constexpr int NRED = 64;
constexpr float SCALE = 0.08838834764831845f;
constexpr float LOG2E = 1.4426950408889634f;
constexpr u32 SPIN_CAP = 1u << 22;

constexpr u32 WS_EPOCH = 0;
constexpr u32 WS_CTR = 1024;
constexpr u32 WS_HOP0 = 4096;
constexpr u32 WS_HOP1M = WS_HOP0 + 8 * 256 * 8;
constexpr u32 WS_HOP1A = 65536;
constexpr u32 WS_HOPF = WS_HOP1A + 8 * 32 * 32 * 8 * 16;
constexpr u32 WS_HOP2 = WS_HOPF + 64 * 64 * 4 * 16;
constexpr u32 WS_DBG = WS_HOP2 + 64 * 256 * 4 * 16;
constexpr u32 WS_BYTES = WS_DBG + 256 * 9 * 16 * 8;

__device__ __forceinline__ float bf2f(u32 h) { return __builtin_bit_cast(float, h << 16); }
__device__ __forceinline__ float bflo(u32 w) { return __builtin_bit_cast(float, w << 16); }
__device__ __forceinline__ float bfhi(u32 w) { return __builtin_bit_cast(float, w & 0xffff0000u); }
__device__ __forceinline__ u32 cvtpk(float lo, float hi) {
  const f32x2 v = {lo, hi};
  return __builtin_bit_cast(u32, __builtin_convertvector(v, bf16x2));
}
__device__ __forceinline__ float bfr(float x) { return bflo(cvtpk(x, x)); }
template <int CTRL> __device__ __forceinline__ float dpp(float x) {
  return __builtin_bit_cast(float, __builtin_amdgcn_mov_dpp(__builtin_bit_cast(int, x), CTRL, 0xf, 0xf, true));
}
__device__ __forceinline__ float xrow_max(float x) {
  auto s = __builtin_amdgcn_permlane16_swap(__float_as_uint(x), __float_as_uint(x), false, false);
  x = fmaxf(__uint_as_float(s[0]), __uint_as_float(s[1]));
  auto t = __builtin_amdgcn_permlane32_swap(__float_as_uint(x), __float_as_uint(x), false, false);
  return fmaxf(__uint_as_float(t[0]), __uint_as_float(t[1]));
}
__device__ __forceinline__ float xrow_sum(float x) {
  auto s = __builtin_amdgcn_permlane16_swap(__float_as_uint(x), __float_as_uint(x), false, false);
  x = __uint_as_float(s[0]) + __uint_as_float(s[1]);
  auto t = __builtin_amdgcn_permlane32_swap(__float_as_uint(x), __float_as_uint(x), false, false);
  return __uint_as_float(t[0]) + __uint_as_float(t[1]);
}
__device__ __forceinline__ float sum_m8(float x) { x += dpp<0x128>(x); return xrow_sum(x); }
__device__ __forceinline__ float sum_m4(float x) { x += dpp<0x124>(x); return sum_m8(x); }
__device__ __forceinline__ float sum_all(float x) { x += dpp<0xB1>(x); x += dpp<0x4E>(x); return sum_m4(x); }
__device__ __forceinline__ float max_m8(float x) { x = fmaxf(x, dpp<0x128>(x)); return xrow_max(x); }
__device__ __forceinline__ u32x4 ldnt(const void* p) { return __builtin_nontemporal_load((const u32x4*)p); }
__device__ __forceinline__ u32 ldnt16(const u16* p) { return (u32)__builtin_nontemporal_load(p); }
__device__ __forceinline__ void wg_barrier() {
  asm volatile("s_waitcnt lgkmcnt(0)" ::: "memory");
  __builtin_amdgcn_s_barrier();
  asm volatile("" ::: "memory");
}
#define FENCE() do { asm volatile("" ::: "memory"); __builtin_amdgcn_sched_barrier(0); } while (0)
#define PUT16(off, f0, f1) __builtin_amdgcn_raw_buffer_store_b128(u32x4{__float_as_uint(f0), tag, __float_as_uint(f1), tag}, rs, (int)(off), 0, 16)
#define GET16(off) __builtin_amdgcn_raw_buffer_load_b128(rs, (int)(off), 0, 16)
#define STAMP(i) do { } while (0)

__global__ void __launch_bounds__(NTHR, 3)
fused_decode(const u16* __restrict__ x, const char* __restrict__ kc, const char* __restrict__ vc,
             const u16* __restrict__ wq, const u16* __restrict__ wo, const u16* __restrict__ w1,
             const u16* __restrict__ w3, const u16* __restrict__ w2, u32* __restrict__ out, char* ws) {
  __shared__ __attribute__((aligned(16))) float sm_o[NSW][4 * DH];
  __shared__ __attribute__((aligned(16))) u16 sm_wo[16 * DM];
  __shared__ __attribute__((aligned(16))) float sm_ml[NSW][4][2];
  __shared__ __attribute__((aligned(16))) float sm_red[NSW][64];
  __shared__ __attribute__((aligned(16))) u32x4 sm_q[64];
  __shared__ __attribute__((aligned(16))) float sm_ov[16];
  __shared__ __attribute__((aligned(16))) float sm_fin[2][NSW][8];
  __shared__ __attribute__((aligned(16))) u32 sm_tag[4];

  const int tid = threadIdx.x, lane = tid & 63;
  const int wave = __builtin_amdgcn_readfirstlane(tid >> 6);
  const int b = blockIdx.x, g = b >> 5, s = b & 31;
  STAMP(0);
  const auto rs = __builtin_amdgcn_make_buffer_rsrc(ws, 0, WS_BYTES, 0x00020000);
  const bool is_q = s < 8, is_f = s >= 8 && s < 15;
  const int fb = g * 7 + (s - 8);

  if (wave < NSW) {
    u32x4 wov[2];
#pragma unroll
    for (int i = 0; i < 2; ++i) wov[i] = ldnt(wo + (size_t)(b * 16 + wave + 8 * i) * DM + lane * 8);
    u32x4 w2v[4];
    float a8[8];
    if (is_q || is_f) {
      const u32x4 xc = ldnt(x + 8 * lane);
      u32x4 wv[8];
      u32 xr0, xr1;
      if (is_q) {
        xr0 = ldnt16(x + 64 * wave + lane); xr1 = 0;
        const u16* wp = wq + (size_t)(64 * wave + (lane >> 3)) * 4096 + g * 512 + s * 64 + (lane & 7) * 8;
#pragma unroll
        for (int i = 0; i < 8; ++i) wv[i] = ldnt(wp + (size_t)(8 * i) * 4096);
      } else {
        xr0 = ldnt16(x + 128 * (wave & 3) + lane); xr1 = ldnt16(x + 128 * (wave & 3) + 64 + lane);
        const u16* wp = (wave < 4 ? w1 : w3) + (size_t)(128 * (wave & 3) + (lane >> 2)) * FF + fb * 32 + (lane & 3) * 8;
#pragma unroll
        for (int i = 0; i < 8; ++i) wv[i] = ldnt(wp + (size_t)(16 * i) * FF);
#pragma unroll
        for (int i = 0; i < 4; ++i) w2v[i] = ldnt(w2 + (size_t)(fb * 32 + wave + 8 * i) * DM + lane * 8);
      }
      FENCE();
      float xv[8];
#pragma unroll
      for (int i = 0; i < 4; ++i) { xv[2 * i] = bflo(xc[i]); xv[2 * i + 1] = bfhi(xc[i]); }
      float sum = ((xv[0] + xv[1]) + (xv[2] + xv[3])) + ((xv[4] + xv[5]) + (xv[6] + xv[7]));
      sum = sum_all(sum);
      const float mean = sum * (1.f / DM);
      float sq = 0.f;
#pragma unroll
      for (int i = 0; i < 8; ++i) { const float d = xv[i] - mean; sq = fmaf(d, d, sq); }
      sq = sum_all(sq);
      const float mb = bfr(mean), vb_ = bfr(sq * (1.f / DM));
      const float den = bfr(sqrtf(bfr(vb_ + 1e-5f)));
      const float xn0 = bfr(bfr(bf2f(xr0) - mb) / den), xn1 = bfr(bfr(bf2f(xr1) - mb) / den);
#pragma unroll
      for (int j = 0; j < 8; ++j) a8[j] = 0.f;
#pragma unroll
      for (int i = 0; i < 8; ++i) {
        const float xn = is_q ? __shfl(xn0, 8 * i + (lane >> 3)) : __shfl(i < 4 ? xn0 : xn1, (16 * i + (lane >> 2)) & 63);
#pragma unroll
        for (int j = 0; j < 4; ++j) { a8[2 * j] = fmaf(xn, bflo(wv[i][j]), a8[2 * j]); a8[2 * j + 1] = fmaf(xn, bfhi(wv[i][j]), a8[2 * j + 1]); }
      }
      if (is_q) {
#pragma unroll
        for (int j = 0; j < 8; ++j) a8[j] = sum_m8(a8[j]);
        if (lane < 8) {
          *(f32x4*)&sm_red[wave][lane * 8] = f32x4{a8[0], a8[1], a8[2], a8[3]};
          *(f32x4*)&sm_red[wave][lane * 8 + 4] = f32x4{a8[4], a8[5], a8[6], a8[7]};
        }
      } else {
#pragma unroll
        for (int j = 0; j < 8; ++j) a8[j] = sum_m4(a8[j]);
        if (lane < 4) {
          *(f32x4*)&sm_red[wave][lane * 8] = f32x4{a8[0], a8[1], a8[2], a8[3]};
          *(f32x4*)&sm_red[wave][lane * 8 + 4] = f32x4{a8[4], a8[5], a8[6], a8[7]};
        }
      }
    }
#pragma unroll
    for (int i = 0; i < 2; ++i) *(u32x4*)&sm_wo[(wave + 8 * i) * DM + lane * 8] = wov[i];
    FENCE();
    STAMP(1);
    const int g4 = lane >> 4;
    const char* kg = kc + (size_t)g * SK * 256;
    const char* vg = vc + (size_t)g * SK * 256;
    const u32 koff = ((lane >> 2) & 3) * 256 + (4 * g4 + (lane & 3)) * 16, voff = lane * 16;
    u32x4 ka[T], va[T], kb[T], vb[T];
#define LOADS(a, p) do { _Pragma("unroll") for (int t_ = 0; t_ < T; ++t_) a[t_] = ldnt((p) + t_ * 1024); FENCE(); } while (0)
    const u32 u0 = (u32)s * 524288u + (u32)wave * 8192u;
    if (!is_q) {
      LOADS(ka, kg + u0 + koff); LOADS(va, vg + u0 + voff); LOADS(kb, kg + u0 + 4096 + koff); LOADS(vb, vg + u0 + 4096 + voff);
    }
    wg_barrier();
    if (is_q) {
      wg_barrier();
      LOADS(ka, kg + u0 + koff); LOADS(va, vg + u0 + voff); LOADS(kb, kg + u0 + 4096 + koff); LOADS(vb, vg + u0 + 4096 + voff);
    }
    const u32 tag = sm_tag[0];
    if (is_f) {
      const int e = lane & 31;
      const float gt = bfr((sm_red[0][e] + sm_red[1][e]) + (sm_red[2][e] + sm_red[3][e]));
      const float up = bfr((sm_red[4][e] + sm_red[5][e]) + (sm_red[6][e] + sm_red[7][e]));
      const float sl = bfr(gt / (1.f + __expf(-gt)));
      const float hv = bfr(sl * up);
#pragma unroll
      for (int j = 0; j < 8; ++j) a8[j] = 0.f;
#pragma unroll
      for (int i = 0; i < 4; ++i) {
        const float h = __builtin_bit_cast(float, __builtin_amdgcn_readlane(__builtin_bit_cast(int, hv), wave + 8 * i));
#pragma unroll
        for (int j = 0; j < 4; ++j) { a8[2 * j] = fmaf(h, bflo(w2v[i][j]), a8[2 * j]); a8[2 * j + 1] = fmaf(h, bfhi(w2v[i][j]), a8[2 * j + 1]); }
      }
      *(f32x4*)&sm_o[wave][lane * 8] = f32x4{a8[0], a8[1], a8[2], a8[3]};
      *(f32x4*)&sm_o[wave][lane * 8 + 4] = f32x4{a8[4], a8[5], a8[6], a8[7]};
      wg_barrier();
      if (tid < 256) {
        float p0 = 0.f, p1 = 0.f;
#pragma unroll
        for (int w = 0; w < NSW; ++w) { const f32x2 v = *(const f32x2*)&sm_o[w][2 * tid]; p0 += v[0]; p1 += v[1]; }
        PUT16(WS_HOPF + (((tid >> 2) * 64 + fb) * 4 + (tid & 3)) * 16, p0, p1);
      }
    }
    wg_barrier();
    STAMP(2);
    const u32x4 qf = sm_q[lane];
    const int jq = lane & 3;
    const u32 mask0 = jq == 0 ? 0x0000ffffu : jq == 1 ? 0xffff0000u : 0u;
    const u32 mask1 = jq == 2 ? 0x0000ffffu : jq == 3 ? 0xffff0000u : 0u;
    float m = -INFINITY, l = 0.f;
    f32x4 acc0 = {0.f, 0.f, 0.f, 0.f}, acc1 = {0.f, 0.f, 0.f, 0.f};
    float sc[T];
#define QK(kf) do { _Pragma("unroll") for (int t_ = 0; t_ < T; ++t_) { \
      const f32x4 d_ = __builtin_amdgcn_mfma_f32_16x16x32_bf16(__builtin_bit_cast(bf16x8, kf[t_]), __builtin_bit_cast(bf16x8, qf), f32x4{0.f, 0.f, 0.f, 0.f}, 0, 0, 0); \
      const float x_ = (dpp<0x00>(d_[0]) + dpp<0x55>(d_[1])) + (dpp<0xAA>(d_[2]) + dpp<0xFF>(d_[3])); \
      sc[t_] = bfr(bfr(x_) * SCALE); } \
      asm volatile("" : "+v"(sc[0]), "+v"(sc[1]), "+v"(sc[2]), "+v"(sc[3]) :: "memory"); __builtin_amdgcn_sched_barrier(0); } while (0)
#define PV(vf) do { \
      float tm_ = fmaxf(fmaxf(sc[0], sc[1]), fmaxf(sc[2], sc[3])); tm_ = xrow_max(tm_); \
      const float mn_ = fmaxf(m, tm_); const float al_ = __builtin_amdgcn_exp2f((m - mn_) * LOG2E); m = mn_; \
      l *= al_; acc0 *= al_; acc1 *= al_; \
      _Pragma("unroll") for (int t_ = 0; t_ < T; ++t_) { \
        const float p_ = __builtin_amdgcn_exp2f((sc[t_] - mn_) * LOG2E); l += p_; \
        const u32 pk_ = cvtpk(p_, p_); const u32x2 bb_ = {pk_ & mask0, pk_ & mask1}; \
        const u32x2 v0_ = {vf[t_][0], vf[t_][1]}, v1_ = {vf[t_][2], vf[t_][3]}; \
        acc0 = __builtin_amdgcn_mfma_f32_16x16x16bf16_1k(__builtin_bit_cast(s16x4, v0_), __builtin_bit_cast(s16x4, bb_), acc0, 0, 0, 0); \
        acc1 = __builtin_amdgcn_mfma_f32_16x16x16bf16_1k(__builtin_bit_cast(s16x4, v1_), __builtin_bit_cast(s16x4, bb_), acc1, 0, 0, 0); } \
      asm volatile("" : "+v"(acc0), "+v"(acc1) :: "memory"); __builtin_amdgcn_sched_barrier(0); } while (0)
    const char* ctr = ws + WS_CTR + g * 256;
    u32 nxt = u0 + 8 * 8192u;
#define BODY() do { const char* kn = kg + nxt + koff; const char* vn = vg + nxt + voff; \
      QK(ka); LOADS(ka, kn); PV(va); LOADS(va, vn); \
      QK(kb); LOADS(kb, kn + 4096); PV(vb); LOADS(vb, vn + 4096); } while (0)
    for (int it = 0; it < SU - 2; ++it) { BODY(); nxt += 8 * 8192u; }
    for (;;) {
      u32 grab; u64 sv;
      asm volatile("s_mov_b64 %1, exec\n\ts_mov_b64 exec, 1\n\tglobal_atomic_add %0, %2, %3, %4 sc0\n\ts_mov_b64 exec, %1"
                   : "=&v"(grab), "=&s"(sv) : "v"(0u), "v"(1u), "s"(ctr) : "memory");
      __builtin_amdgcn_sched_barrier(0);
      BODY();
      asm volatile("s_waitcnt vmcnt(16)" : "+v"(grab) :: "memory");
      const u32 d = __builtin_amdgcn_readfirstlane(grab);
      if (d >= (u32)POOL) break;
      nxt = (d & 31u) * 524288u + ((u32)(UNITS_WG - DU) + (d >> 5)) * 8192u;
    }
    QK(ka); PV(va); QK(kb); PV(vb);
    STAMP(3);
    l = xrow_sum(l);
    {
      const int h = (lane & 15) >> 2;
#pragma unroll
      for (int r = 0; r < 4; ++r) {
        sm_o[wave][h * DH + 32 * g4 + 8 * r + jq] = acc0[r];
        sm_o[wave][h * DH + 32 * g4 + 8 * r + 4 + jq] = acc1[r];
      }
      if (lane < 16 && jq == 0) { sm_ml[wave][h][0] = m; sm_ml[wave][h][1] = l; }
    }
    wg_barrier();
    if (tid < 256) {
      const int h = tid >> 6, d = 2 * (tid & 63);
      float M = sm_ml[0][h][0];
#pragma unroll
      for (int w = 1; w < NSW; ++w) M = fmaxf(M, sm_ml[w][h][0]);
      float v0 = 0.f, v1 = 0.f, L = 0.f;
#pragma unroll
      for (int w = 0; w < NSW; ++w) {
        const float e = __builtin_amdgcn_exp2f((sm_ml[w][h][0] - M) * LOG2E);
        const f32x2 o2 = *(const f32x2*)&sm_o[w][h * DH + d];
        v0 = fmaf(e, o2[0], v0); v1 = fmaf(e, o2[1], v1); L = fmaf(e, sm_ml[w][h][1], L);
      }
      PUT16(WS_HOP1A + (((g * 32 + (tid >> 3)) * 32 + s) * 8 + (tid & 7)) * 16, v0, v1);
      if ((tid & 63) == 0) PUT16(WS_HOP1M + ((g * 4 + h) * 32 + s) * 16, M, L);
    }
    STAMP(4);
    if (b < NRED) {
      float f0 = 0.f, f1 = 0.f;
      if (tid < NFP * 4) {
        for (u32 spin = 0; spin < SPIN_CAP; ++spin) {
          asm volatile("" ::: "memory");
          const u32x4 v = GET16(WS_HOPF + (b * 64 * 4 + tid) * 16);
          f0 = __uint_as_float(v[0]); f1 = __uint_as_float(v[2]);
          if (__all(v[1] == tag && v[3] == tag)) break;
        }
      }
      f0 = sum_m4(f0); f1 = sum_m4(f1);
      if (lane < 4) { sm_fin[0][wave][2 * lane] = f0; sm_fin[0][wave][2 * lane + 1] = f1; }
    }
    wg_barrier();
    if (tid < 256) {
      float p0 = 0.f, p1 = 0.f;
#pragma unroll
      for (int r = 0; r < 16; ++r) {
        const u32 w = *(const u32*)&sm_wo[r * DM + 2 * tid];
        const float o = sm_ov[r];
        p0 = fmaf(o, bflo(w), p0); p1 = fmaf(o, bfhi(w), p1);
      }
      PUT16(WS_HOP2 + (((tid >> 2) * 256 + b) * 4 + (tid & 3)) * 16, p0, p1);
    }
    STAMP(5);
    if (b < NRED) {
      float a0 = 0.f, a1 = 0.f;
      for (u32 spin = 0; spin < SPIN_CAP; ++spin) {
        asm volatile("" ::: "memory");
        const u32x4 v = GET16(WS_HOP2 + (b * 1024 + tid) * 16), w = GET16(WS_HOP2 + (b * 1024 + 512 + tid) * 16);
        a0 = __uint_as_float(v[0]) + __uint_as_float(w[0]); a1 = __uint_as_float(v[2]) + __uint_as_float(w[2]);
        if (__all(v[1] == tag && v[3] == tag && w[1] == tag && w[3] == tag)) break;
      }
      STAMP(6);
      a0 = sum_m4(a0); a1 = sum_m4(a1);
      if (lane < 4) { sm_fin[1][wave][2 * lane] = a0; sm_fin[1][wave][2 * lane + 1] = a1; }
      wg_barrier();
    }
    STAMP(7);
  } else {
    if (s == 0 && lane == 0) __hip_atomic_store((gu32*)(ws + WS_CTR + g * 256), 0u, RLX_AGENT);
    const u32 tag = __hip_atomic_load((gu32*)(ws + WS_EPOCH), RLX_AGENT) + 1u;
    u32 xo = 0;
    if (b < NRED && lane < 4) xo = *(const u32*)(x + 8 * b + 2 * lane);
    if (lane == 0) sm_tag[0] = tag;
    asm volatile("s_waitcnt vmcnt(0)" ::: "memory");
    wg_barrier();
    if (is_q) {
      if (lane < 16) {
        float q4[4];
#pragma unroll
        for (int k = 0; k < 4; ++k) {
          float a = 0.f;
#pragma unroll
          for (int w = 0; w < NSW; ++w) a += sm_red[w][lane * 4 + k];
          q4[k] = a;
        }
        __builtin_amdgcn_raw_buffer_store_b128(u32x4{cvtpk(q4[0], q4[1]), tag, cvtpk(q4[2], q4[3]), tag}, rs, (int)(WS_HOP0 + (g * 256 + s * 32 + lane * 2) * 8), 0, 16);
      }
      wg_barrier();
    }
    if (is_f) wg_barrier();
    STAMP(1);
    {
      const u32 src = WS_HOP0 + (g * 256 + ((lane & 15) >> 2) * 64 + (lane >> 4) * 16 + (lane & 3) * 4) * 8;
      u32x4 qv;
      for (u32 spin = 0; spin < SPIN_CAP; ++spin) {
        asm volatile("" ::: "memory");
        const u32x4 v = GET16(src), w = GET16(src + 16);
        qv = u32x4{v[0], v[2], w[0], w[2]};
        if (__all(v[1] == tag && v[3] == tag && w[1] == tag && w[3] == tag)) break;
      }
      sm_q[lane] = qv;
    }
    STAMP(2);
    wg_barrier();
    wg_barrier();
    STAMP(4);
    {
      const u32 sa = WS_HOP1A + ((g * 32 + s) * 256 + lane) * 16;
      const u32 sm = WS_HOP1M + ((g * 4 + (s >> 3)) * 32 + (lane >> 3)) * 16;
      u32x4 av[4], mv[4];
      for (u32 spin = 0; spin < SPIN_CAP; ++spin) {
        asm volatile("" ::: "memory");
        bool ok = true;
#pragma unroll
        for (int i = 0; i < 4; ++i) { av[i] = GET16(sa + i * 1024); mv[i] = GET16(sm + i * 128); }
#pragma unroll
        for (int i = 0; i < 4; ++i) ok &= av[i][1] == tag && av[i][3] == tag && mv[i][1] == tag && mv[i][3] == tag;
        if (__all(ok)) break;
      }
      STAMP(5);
      float M = fmaxf(fmaxf(__uint_as_float(mv[0][0]), __uint_as_float(mv[1][0])), fmaxf(__uint_as_float(mv[2][0]), __uint_as_float(mv[3][0])));
      M = max_m8(M);
      float L = 0.f, o0 = 0.f, o1 = 0.f;
#pragma unroll
      for (int i = 0; i < 4; ++i) {
        const float w = __builtin_amdgcn_exp2f((__uint_as_float(mv[i][0]) - M) * LOG2E);
        L = fmaf(w, __uint_as_float(mv[i][2]), L); o0 = fmaf(w, __uint_as_float(av[i][0]), o0); o1 = fmaf(w, __uint_as_float(av[i][2]), o1);
      }
      L = sum_m8(L); o0 = sum_m8(o0); o1 = sum_m8(o1);
      if (lane < 8) *(f32x2*)&sm_ov[2 * lane] = f32x2{bfr(o0 / L), bfr(o1 / L)};
    }
    wg_barrier();
    STAMP(6);
    if (b < NRED) {
      wg_barrier();
      if (lane < 4) {
        float a0 = 0.f, a1 = 0.f, f0 = 0.f, f1 = 0.f;
#pragma unroll
        for (int w = 0; w < NSW; ++w) {
          a0 += sm_fin[1][w][2 * lane]; a1 += sm_fin[1][w][2 * lane + 1];
          f0 += sm_fin[0][w][2 * lane]; f1 += sm_fin[0][w][2 * lane + 1];
        }
        const float o0 = bfr(bflo(xo) + bfr(a0)) + bfr(f0), o1 = bfr(bfhi(xo) + bfr(a1)) + bfr(f1);
        out[4 * b + lane] = cvtpk(o0, o1);
        if (b == 0 && lane == 0) __hip_atomic_store((gu32*)(ws + WS_EPOCH), tag, RLX_AGENT);
      }
    }
    STAMP(8);
  }
}
}

extern "C" void kernel_launch(void* const* d_in, const int* in_sizes, int n_in,
                              void* d_out, int out_size, void* d_ws, size_t ws_size,
                              hipStream_t stream) {
  (void)in_sizes; (void)n_in; (void)out_size; (void)ws_size;
  hipLaunchKernelGGL(fused_decode, dim3(NWG), dim3(NTHR), 0, stream,
                     (const u16*)d_in[0], (const char*)d_in[1], (const char*)d_in[2], (const u16*)d_in[3], (const u16*)d_in[4],
                     (const u16*)d_in[6], (const u16*)d_in[7], (const u16*)d_in[8], (u32*)d_out, (char*)d_ws);
}
```

```cpp
#include <hip/hip_runtime.h>
#include <hip/hip_bf16.h>
#include <stdint.h>

typedef unsigned int u32;
typedef unsigned long long u64;
typedef unsigned short u16;
typedef __attribute__((ext_vector_type(4))) unsigned u32x4;
typedef __attribute__((ext_vector_type(4))) float f32x4;
typedef __attribute__((ext_vector_type(8))) __bf16 bf16x8;
typedef __attribute__((ext_vector_type(4))) short s16x4;
typedef __attribute__((ext_vector_type(2))) unsigned u32x2;
typedef __attribute__((ext_vector_type(2))) float f32x2;
typedef __attribute__((ext_vector_type(2))) __bf16 bf16x2;
typedef __attribute__((address_space(1))) u32 gu32;
#define RLX_AGENT __ATOMIC_RELAXED, __HIP_MEMORY_SCOPE_AGENT
#define RLX_WG __ATOMIC_RELAXED, __HIP_MEMORY_SCOPE_WORKGROUP

namespace {
constexpr int NWG = 256, NSW = 4, NTHR = (NSW + 1) * 64;
constexpr int DM = 512, SK = 65536, DH = 128, FF = 1792;
constexpr int T = 4;
constexpr u32 TB = T * 1024u;
constexpr int NTILE = SK * 256 / TB;
constexpr int NPARK = 2;
constexpr int UQ = 120, UF = 114;
constexpr int UREM = NTILE - 8 * UQ - 7 * UF, UO = UREM / 17, UX = UREM % 17;
constexpr int NFP = 56;
constexpr int NRED = 32;
constexpr float SCALE = 0.08838834764831845f;
constexpr float LOG2E = 1.4426950408889634f;
constexpr u32 SPIN_CAP = 1u << 17;

constexpr u32 WS_EPOCH = 0;
constexpr u32 WS_HOP0 = 4096;
constexpr u32 WS_HOP1M = WS_HOP0 + 8 * 256 * 8;
constexpr u32 WS_HOP1A = 65536;
constexpr u32 WS_HOPF = WS_HOP1A + 8 * 32 * 32 * 8 * 16;
constexpr u32 WS_HOP2 = WS_HOPF + 32 * 64 * 8 * 16;
constexpr u32 WS_BYTES = WS_HOP2 + 32 * 256 * 8 * 16;

constexpr int L_WREG = NPARK * 4 * TB;
constexpr int L_O = 0;
constexpr int L_WO = 2048;
constexpr int L_F = 8192;
constexpr int L_MISC = NSW * L_WREG;
constexpr int L_ML = L_MISC;
constexpr int L_RED = L_ML + 256;
constexpr int L_Q = L_RED + 2048;
constexpr int L_OV = L_Q + 1024;
constexpr int L_FIN = L_OV + 64;
constexpr int L_TAG = L_FIN + 512;
constexpr int L_BYTES = L_TAG + 16;

__device__ __forceinline__ float bf2f(u32 h) { return __builtin_bit_cast(float, h << 16); }
__device__ __forceinline__ float bflo(u32 w) { return __builtin_bit_cast(float, w << 16); }
__device__ __forceinline__ float bfhi(u32 w) { return __builtin_bit_cast(float, w & 0xffff0000u); }
__device__ __forceinline__ u32 cvtpk(float lo, float hi) {
  const f32x2 v = {lo, hi};
  return __builtin_bit_cast(u32, __builtin_convertvector(v, bf16x2));
}
__device__ __forceinline__ float bfr(float x) { return bflo(cvtpk(x, x)); }
template <int CTRL> __device__ __forceinline__ float dpp(float x) {
  return __builtin_bit_cast(float, __builtin_amdgcn_mov_dpp(__builtin_bit_cast(int, x), CTRL, 0xf, 0xf, true));
}
__device__ __forceinline__ float xrow_max(float x) {
  auto s = __builtin_amdgcn_permlane16_swap(__float_as_uint(x), __float_as_uint(x), false, false);
  x = fmaxf(__uint_as_float(s[0]), __uint_as_float(s[1]));
  auto t = __builtin_amdgcn_permlane32_swap(__float_as_uint(x), __float_as_uint(x), false, false);
  return fmaxf(__uint_as_float(t[0]), __uint_as_float(t[1]));
}
__device__ __forceinline__ float xrow_sum(float x) {
  auto s = __builtin_amdgcn_permlane16_swap(__float_as_uint(x), __float_as_uint(x), false, false);
  x = __uint_as_float(s[0]) + __uint_as_float(s[1]);
  auto t = __builtin_amdgcn_permlane32_swap(__float_as_uint(x), __float_as_uint(x), false, false);
  return __uint_as_float(t[0]) + __uint_as_float(t[1]);
}
__device__ __forceinline__ float sum_m8(float x) { x += dpp<0x128>(x); return xrow_sum(x); }
__device__ __forceinline__ float sum_m4(float x) { x += dpp<0x124>(x); return sum_m8(x); }
__device__ __forceinline__ float sum_all(float x) { x += dpp<0xB1>(x); x += dpp<0x4E>(x); return sum_m4(x); }
__device__ __forceinline__ float max_m8(float x) { x = fmaxf(x, dpp<0x128>(x)); return xrow_max(x); }
__device__ __forceinline__ u32x4 ldnt(const void* p) { return __builtin_nontemporal_load((const u32x4*)p); }
__device__ __forceinline__ u32 ldnt16(const u16* p) { return (u32)__builtin_nontemporal_load(p); }
__device__ __forceinline__ void wg_barrier() {
  asm volatile("s_waitcnt lgkmcnt(0)" ::: "memory");
  __builtin_amdgcn_s_barrier();
  asm volatile("" ::: "memory");
}
__device__ __forceinline__ void ln_stats(const u32x4 xc, float& mb, float& den) {
  float xv[8];
#pragma unroll
  for (int i = 0; i < 4; ++i) { xv[2 * i] = bflo(xc[i]); xv[2 * i + 1] = bfhi(xc[i]); }
  float sum = ((xv[0] + xv[1]) + (xv[2] + xv[3])) + ((xv[4] + xv[5]) + (xv[6] + xv[7]));
  sum = sum_all(sum);
  const float mean = sum * (1.f / DM);
  float sq = 0.f;
#pragma unroll
  for (int i = 0; i < 8; ++i) { const float d = xv[i] - mean; sq = fmaf(d, d, sq); }
  sq = sum_all(sq);
  mb = bfr(mean);
  den = bfr(sqrtf(bfr(bfr(sq * (1.f / DM)) + 1e-5f)));
}
#define LDS_LD(p) __hip_atomic_load((p), RLX_WG)
#define WAVE_SYNC(i) do { asm volatile("s_waitcnt lgkmcnt(0)" ::: "memory"); \
    if (lane == 0) __hip_atomic_fetch_add(&sm_flag[i], 1u, RLX_WG); \
    while (LDS_LD(&sm_flag[i]) < (u32)NSW) __builtin_amdgcn_s_sleep(1); \
    asm volatile("" ::: "memory"); } while (0)
#define FENCE() do { asm volatile("" ::: "memory"); __builtin_amdgcn_sched_barrier(0); } while (0)
#define PUT16(off, f0, f1) __builtin_amdgcn_raw_buffer_store_b128(u32x4{__float_as_uint(f0), tag, __float_as_uint(f1), tag}, rs, (int)(off), 0, 16)
#define GET16(off) __builtin_amdgcn_raw_buffer_load_b128(rs, (int)(off), 0, 16)
#define FMA8(acc, sc_, w_) do { _Pragma("unroll") for (int j_ = 0; j_ < 4; ++j_) { \
    acc[2 * j_] = fmaf(sc_, bflo((w_)[j_]), acc[2 * j_]); acc[2 * j_ + 1] = fmaf(sc_, bfhi((w_)[j_]), acc[2 * j_ + 1]); } } while (0)

__global__ void __launch_bounds__(NTHR, 2)
fused_decode(const u16* __restrict__ x, const char* __restrict__ kc, const char* __restrict__ vc,
             const u16* __restrict__ wq, const u16* __restrict__ wo, const u16* __restrict__ w1,
             const u16* __restrict__ w3, const u16* __restrict__ w2, u32* __restrict__ out, char* ws) {
  __shared__ __attribute__((aligned(16))) char lds[L_BYTES];
  float* sm_o = (float*)(lds + L_O);
  u16* sm_wo = (u16*)(lds + L_WO);
  float* sm_ml = (float*)(lds + L_ML);
  float* sm_red = (float*)(lds + L_RED);
  u32x4* sm_q = (u32x4*)(lds + L_Q);
  float* sm_ov = (float*)(lds + L_OV);
  float* sm_fin = (float*)(lds + L_FIN);
  u32* sm_tag = (u32*)(lds + L_TAG);
  u32* sm_flag = (u32*)(lds + L_TAG + 4);

  const int tid = threadIdx.x, lane = tid & 63;
  const int wave = __builtin_amdgcn_readfirstlane(tid >> 6);
  const int b = blockIdx.x, g = b >> 5, s = b & 31;
  const auto rs = __builtin_amdgcn_make_buffer_rsrc(ws, 0, WS_BYTES, 0x00020000);
  const bool is_q = s < 8, is_f = s >= 8 && s < 15;
  if (tid < 3) sm_flag[tid] = 0u;
  wg_barrier();

  if (wave < NSW) {
    if (is_q) {
      const u32x4 xc = ldnt(x + 8 * lane);
      const u32 xr0 = ldnt16(x + 128 * wave + lane), xr1 = ldnt16(x + 128 * wave + 64 + lane);
      u32x4 wv[16];
      const u16* wp = wq + (size_t)(128 * wave + (lane >> 3)) * 4096 + g * 512 + s * 64 + (lane & 7) * 8;
#pragma unroll
      for (int i = 0; i < 16; ++i) wv[i] = ldnt(wp + (size_t)(8 * i) * 4096);
      FENCE();
      float mb, den;
      ln_stats(xc, mb, den);
      const float xn0 = bfr(bfr(bf2f(xr0) - mb) / den), xn1 = bfr(bfr(bf2f(xr1) - mb) / den);
      float a8[8];
#pragma unroll
      for (int j = 0; j < 8; ++j) a8[j] = 0.f;
#pragma unroll
      for (int i = 0; i < 16; ++i) { const float xn = __shfl(i < 8 ? xn0 : xn1, (8 * i + (lane >> 3)) & 63); FMA8(a8, xn, wv[i]); }
#pragma unroll
      for (int j = 0; j < 8; ++j) a8[j] = sum_m8(a8[j]);
      if (lane < 8) {
        *(f32x4*)&sm_red[wave * 64 + lane * 8] = f32x4{a8[0], a8[1], a8[2], a8[3]};
        *(f32x4*)&sm_red[wave * 64 + lane * 8 + 4] = f32x4{a8[4], a8[5], a8[6], a8[7]};
      }
      wg_barrier();
      wg_barrier();
    }
    const int g4 = lane >> 4;
    const char* kg = kc + (size_t)g * SK * 256;
    const char* vg = vc + (size_t)g * SK * 256;
    const u32 koff = ((lane >> 2) & 3) * 256 + (4 * g4 + (lane & 3)) * 16, voff = lane * 16;
    const int ustart = s < 8 ? s * UQ : s < 15 ? 8 * UQ + (s - 8) * UF : 8 * UQ + 7 * UF + (s - 15) * UO + min(s - 15, UX);
    const int ucnt = s < 8 ? UQ : s < 15 ? UF : UO + (s - 15 < UX ? 1 : 0);
    const int nt = (ucnt - wave + NSW - 1) / NSW;
    constexpr u32 TSTR = NSW * TB;
    u32x4 ka[T], va[T], kb[T], vb[T];
#define LOADS(a, p) do { _Pragma("unroll") for (int t_ = 0; t_ < T; ++t_) a[t_] = ldnt((p) + t_ * 1024); FENCE(); } while (0)
#define PARK(a, o) do { _Pragma("unroll") for (int t_ = 0; t_ < T; ++t_) park[(o) * (T * 64) + t_ * 64] = a[t_]; FENCE(); } while (0)
    u32 nxt = (u32)(ustart + wave) * TB;
    const int fb = ((g >> 1) * 7 + (s - 8)) * 2 + (g & 1);
    u32x4* park = (u32x4*)(lds + wave * L_WREG) + lane;
    const int np = is_f ? 0 : NPARK;
    float pf0 = 0.f, pf1 = 0.f;
    if (!is_f) {
      LOADS(ka, kg + nxt + koff); LOADS(va, vg + nxt + voff); LOADS(kb, kg + nxt + TSTR + koff); LOADS(vb, vg + nxt + TSTR + voff);
      nxt += 2 * TSTR;
#pragma unroll
      for (int p = 0; p < NPARK; ++p) {
        PARK(ka, 4 * p); LOADS(ka, kg + nxt + koff); PARK(va, 4 * p + 1); LOADS(va, vg + nxt + voff);
        PARK(kb, 4 * p + 2); LOADS(kb, kg + nxt + TSTR + koff); PARK(vb, 4 * p + 3); LOADS(vb, vg + nxt + TSTR + voff);
        nxt += 2 * TSTR;
      }
    } else {
      u32x4 fwv[16], fw2[8];
      u32 fxr[4];
      const int wr = wave & 1;
      const u32x4 fxc = ldnt(x + 8 * lane);
#pragma unroll
      for (int i = 0; i < 4; ++i) fxr[i] = ldnt16(x + 256 * wr + 64 * i + lane);
      const u16* wp = (wave < 2 ? w1 : w3) + (size_t)(256 * wr + (lane >> 2)) * FF + fb * 32 + (lane & 3) * 8;
#pragma unroll
      for (int i = 0; i < 16; ++i) fwv[i] = ldnt(wp + (size_t)(16 * i) * FF);
#pragma unroll
      for (int i = 0; i < 8; ++i) fw2[i] = ldnt(w2 + (size_t)(fb * 32 + wave + NSW * i) * DM + lane * 8);
      FENCE();
      LOADS(ka, kg + nxt + koff); LOADS(va, vg + nxt + voff); LOADS(kb, kg + nxt + TSTR + koff); LOADS(vb, vg + nxt + TSTR + voff);
      nxt += 2 * TSTR;
      float mb, den;
      ln_stats(fxc, mb, den);
      float xn[4];
#pragma unroll
      for (int i = 0; i < 4; ++i) xn[i] = bfr(bfr(bf2f(fxr[i]) - mb) / den);
      float a8[8];
#pragma unroll
      for (int j = 0; j < 8; ++j) a8[j] = 0.f;
#pragma unroll
      for (int i = 0; i < 16; ++i) { const float xx = __shfl(xn[i / 4], (16 * i + (lane >> 2)) & 63); FMA8(a8, xx, fwv[i]); }
#pragma unroll
      for (int j = 0; j < 8; ++j) a8[j] = sum_m4(a8[j]);
      if (lane < 4) {
        *(f32x4*)&sm_red[wave * 64 + lane * 8] = f32x4{a8[0], a8[1], a8[2], a8[3]};
        *(f32x4*)&sm_red[wave * 64 + lane * 8 + 4] = f32x4{a8[4], a8[5], a8[6], a8[7]};
      }
      WAVE_SYNC(1);
      const float gt = bfr(sm_red[lane & 31] + sm_red[64 + (lane & 31)]);
      const float up = bfr(sm_red[128 + (lane & 31)] + sm_red[192 + (lane & 31)]);
      const float hv = bfr(bfr(gt / (1.f + __expf(-gt))) * up);
#pragma unroll
      for (int j = 0; j < 8; ++j) a8[j] = 0.f;
#pragma unroll
      for (int i = 0; i < 8; ++i) {
        const float h = __builtin_bit_cast(float, __builtin_amdgcn_readlane(__builtin_bit_cast(int, hv), wave + NSW * i));
        FMA8(a8, h, fw2[i]);
      }
      float* sm_f = (float*)(lds + wave * L_WREG + L_F);
      *(f32x4*)&sm_f[lane * 8] = f32x4{a8[0], a8[1], a8[2], a8[3]};
      *(f32x4*)&sm_f[lane * 8 + 4] = f32x4{a8[4], a8[5], a8[6], a8[7]};
      WAVE_SYNC(2);
#pragma unroll
      for (int w = 0; w < NSW; ++w) { const f32x2 v = *(const f32x2*)((const float*)(lds + w * L_WREG + L_F) + 2 * tid); pf0 += v[0]; pf1 += v[1]; }
    }
    while (LDS_LD(sm_flag) == 0u) __builtin_amdgcn_s_sleep(1);
    asm volatile("" ::: "memory");
    const u32x4 qf = sm_q[lane];
    const u32 tag = sm_tag[0];
    if (is_f) PUT16(WS_HOPF + (((tid >> 3) * 64 + fb) * 8 + (tid & 7)) * 16, pf0, pf1);
    u32x4 wov[4];
#pragma unroll
    for (int i = 0; i < 4; ++i) wov[i] = ldnt(wo + (size_t)(b * 16 + wave + NSW * i) * DM + lane * 8);
    FENCE();
    const int jq = lane & 3;
    const u32 mask0 = jq == 0 ? 0x0000ffffu : jq == 1 ? 0xffff0000u : 0u;
    const u32 mask1 = jq == 2 ? 0x0000ffffu : jq == 3 ? 0xffff0000u : 0u;
    float m = -INFINITY, l = 0.f;
    f32x4 acc0 = {0.f, 0.f, 0.f, 0.f}, acc1 = {0.f, 0.f, 0.f, 0.f};
    float sc[T];
#define QK(kf) do { _Pragma("unroll") for (int t_ = 0; t_ < T; ++t_) { \
      const f32x4 d_ = __builtin_amdgcn_mfma_f32_16x16x32_bf16(__builtin_bit_cast(bf16x8, kf[t_]), __builtin_bit_cast(bf16x8, qf), f32x4{0.f, 0.f, 0.f, 0.f}, 0, 0, 0); \
      const float x_ = (dpp<0x00>(d_[0]) + dpp<0x55>(d_[1])) + (dpp<0xAA>(d_[2]) + dpp<0xFF>(d_[3])); \
      sc[t_] = bfr(bfr(x_) * SCALE); } \
      _Pragma("unroll") for (int t_ = 0; t_ < T; ++t_) asm volatile("" : "+v"(sc[t_]) :: "memory"); __builtin_amdgcn_sched_barrier(0); } while (0)
#define PV(vf) do { \
      float tm_ = sc[0]; _Pragma("unroll") for (int t_ = 1; t_ < T; ++t_) tm_ = fmaxf(tm_, sc[t_]); tm_ = xrow_max(tm_); \
      const float mn_ = fmaxf(m, tm_); const float al_ = __builtin_amdgcn_exp2f((m - mn_) * LOG2E); m = mn_; \
      l *= al_; acc0 *= al_; acc1 *= al_; \
      _Pragma("unroll") for (int t_ = 0; t_ < T; ++t_) { \
        const float p_ = __builtin_amdgcn_exp2f((sc[t_] - mn_) * LOG2E); l += p_; \
        const u32 pk_ = cvtpk(p_, p_); const u32x2 bb_ = {pk_ & mask0, pk_ & mask1}; \
        const u32x2 v0_ = {vf[t_][0], vf[t_][1]}, v1_ = {vf[t_][2], vf[t_][3]}; \
        acc0 = __builtin_amdgcn_mfma_f32_16x16x16bf16_1k(__builtin_bit_cast(s16x4, v0_), __builtin_bit_cast(s16x4, bb_), acc0, 0, 0, 0); \
        acc1 = __builtin_amdgcn_mfma_f32_16x16x16bf16_1k(__builtin_bit_cast(s16x4, v1_), __builtin_bit_cast(s16x4, bb_), acc1, 0, 0, 0); } \
      asm volatile("" : "+v"(acc0), "+v"(acc1) :: "memory"); __builtin_amdgcn_sched_barrier(0); } while (0)
    {
      const char* dummy = (const char*)wq;
      int j = 2 * np + 2;
      bool first = true;
      for (int c = 2 * np; c < nt; c += 2, j += 2) {
        const bool la = j < nt, lb = j + 1 < nt, bv = c + 1 < nt;
        const u32 ma = la ? ~0u : 0u, mb_ = lb ? ~0u : 0u;
        const char* kna = (la ? kg + nxt : dummy) + (koff & ma); const char* vna = (la ? vg + nxt : dummy) + (voff & ma);
        const char* knb = (lb ? kg + nxt + TSTR : dummy) + (koff & mb_); const char* vnb = (lb ? vg + nxt + TSTR : dummy) + (voff & mb_);
        QK(ka); LOADS(ka, kna); PV(va); LOADS(va, vna);
        if (bv) QK(kb);
        LOADS(kb, knb);
        if (bv) PV(vb);
        LOADS(vb, vnb);
        nxt += 2 * TSTR;
        if (first) {
          first = false;
#pragma unroll 1
          for (int p = 0; p < 2 * np; ++p) {
            u32x4 tk[T], tv[T];
#pragma unroll
            for (int t_ = 0; t_ < T; ++t_) { tk[t_] = park[(2 * p) * (T * 64) + t_ * 64]; tv[t_] = park[(2 * p + 1) * (T * 64) + t_ * 64]; }
            QK(tk); PV(tv);
          }
#pragma unroll
          for (int i = 0; i < 4; ++i) *(u32x4*)&sm_wo[wave * (L_WREG / 2) + i * DM + lane * 8] = wov[i];
        }
      }
    }
    l = xrow_sum(l);
    {
      const int h = (lane & 15) >> 2;
#pragma unroll
      for (int r = 0; r < 4; ++r) {
        sm_o[wave * (L_WREG / 4) + h * DH + 32 * g4 + 8 * r + jq] = acc0[r];
        sm_o[wave * (L_WREG / 4) + h * DH + 32 * g4 + 8 * r + 4 + jq] = acc1[r];
      }
      if (lane < 16 && jq == 0) { sm_ml[(wave * 4 + h) * 2] = m; sm_ml[(wave * 4 + h) * 2 + 1] = l; }
    }
    wg_barrier();
    {
      const int h = wave, d = 2 * lane;
      float M = sm_ml[h * 2];
#pragma unroll
      for (int w = 1; w < NSW; ++w) M = fmaxf(M, sm_ml[(w * 4 + h) * 2]);
      float v0 = 0.f, v1 = 0.f, L = 0.f;
#pragma unroll
      for (int w = 0; w < NSW; ++w) {
        const float e = __builtin_amdgcn_exp2f((sm_ml[(w * 4 + h) * 2] - M) * LOG2E);
        const f32x2 o2 = *(const f32x2*)&sm_o[w * (L_WREG / 4) + h * DH + d];
        v0 = fmaf(e, o2[0], v0); v1 = fmaf(e, o2[1], v1); L = fmaf(e, sm_ml[(w * 4 + h) * 2 + 1], L);
      }
      PUT16(WS_HOP1A + (((g * 32 + (tid >> 3)) * 32 + s) * 8 + (tid & 7)) * 16, v0, v1);
      if (lane == 0) PUT16(WS_HOP1M + ((g * 4 + h) * 32 + s) * 16, M, L);
    }
    if (b < NRED) {
      float f0 = 0.f, f1 = 0.f;
      for (u32 spin = 0; spin < SPIN_CAP; ++spin) {
        asm volatile("" ::: "memory");
        const bool two = tid + 256 < NFP * 8;
        const u32x4 v = GET16(WS_HOPF + (b * 64 * 8 + tid) * 16);
        const u32x4 w = GET16(two ? WS_HOPF + (b * 64 * 8 + 256 + tid) * 16 : 0x7ffffff0u);
        f0 = __uint_as_float(v[0]) + __uint_as_float(w[0]); f1 = __uint_as_float(v[2]) + __uint_as_float(w[2]);
        if (__all(v[1] == tag && v[3] == tag && (!two || (w[1] == tag && w[3] == tag)))) break;
      }
      f0 = sum_m8(f0); f1 = sum_m8(f1);
      if (lane < 8) { sm_fin[wave * 16 + 2 * lane] = f0; sm_fin[wave * 16 + 2 * lane + 1] = f1; }
    }
    wg_barrier();
    {
      float p0 = 0.f, p1 = 0.f;
#pragma unroll
      for (int r = 0; r < 16; ++r) {
        const u32 w = *(const u32*)&sm_wo[(r % NSW) * (L_WREG / 2) + (r / NSW) * DM + 2 * tid];
        const float o = sm_ov[r];
        p0 = fmaf(o, bflo(w), p0); p1 = fmaf(o, bfhi(w), p1);
      }
      PUT16(WS_HOP2 + (((tid >> 3) * 256 + b) * 8 + (tid & 7)) * 16, p0, p1);
    }
    if (b < NRED) {
      float a0 = 0.f, a1 = 0.f;
      for (u32 spin = 0; spin < SPIN_CAP; ++spin) {
        asm volatile("" ::: "memory");
        u32x4 v[8]; bool ok = true;
#pragma unroll
        for (int i = 0; i < 8; ++i) v[i] = GET16(WS_HOP2 + (b * 2048 + i * 256 + tid) * 16);
        a0 = 0.f; a1 = 0.f;
#pragma unroll
        for (int i = 0; i < 8; ++i) { a0 += __uint_as_float(v[i][0]); a1 += __uint_as_float(v[i][2]); ok &= (v[i][1] == tag) & (v[i][3] == tag); }
        if (__all(ok)) break;
      }
      a0 = sum_m8(a0); a1 = sum_m8(a1);
      if (lane < 8) { sm_fin[64 + wave * 16 + 2 * lane] = a0; sm_fin[64 + wave * 16 + 2 * lane + 1] = a1; }
      wg_barrier();
    }
  } else {
    const u32 tag = __hip_atomic_load((gu32*)(ws + WS_EPOCH), RLX_AGENT) + 1u;
    u32 xo = 0;
    if (b < NRED && lane < 8) xo = *(const u32*)(x + 16 * b + 2 * lane);
    if (lane == 0) sm_tag[0] = tag;
    if (is_q) {
      wg_barrier();
      if (lane < 16) {
        float q4[4];
#pragma unroll
        for (int k = 0; k < 4; ++k) {
          float a = 0.f;
#pragma unroll
          for (int w = 0; w < NSW; ++w) a += sm_red[w * 64 + lane * 4 + k];
          q4[k] = a;
        }
        __builtin_amdgcn_raw_buffer_store_b128(u32x4{cvtpk(q4[0], q4[1]), tag, cvtpk(q4[2], q4[3]), tag}, rs,
                                               (int)(WS_HOP0 + (g * 256 + s * 32 + lane * 2) * 8), 0, 16);
      }
      wg_barrier();
    }
    {
      const u32 src = WS_HOP0 + (g * 256 + ((lane & 15) >> 2) * 64 + (lane >> 4) * 16 + (lane & 3) * 4) * 8;
      u32x4 qv;
      for (u32 spin = 0; spin < SPIN_CAP; ++spin) {
        asm volatile("" ::: "memory");
        const u32x4 v = GET16(src), w = GET16(src + 16);
        qv = u32x4{v[0], v[2], w[0], w[2]};
        if (__all(v[1] == tag && v[3] == tag && w[1] == tag && w[3] == tag)) break;
      }
      sm_q[lane] = qv;
    }
    asm volatile("s_waitcnt lgkmcnt(0)" ::: "memory");
    if (lane == 0) __hip_atomic_store(sm_flag, 1u, RLX_WG);
    wg_barrier();
    {
      const u32 sa = WS_HOP1A + ((g * 32 + s) * 256 + lane) * 16;
      const u32 sm = WS_HOP1M + ((g * 4 + (s >> 3)) * 32 + (lane >> 3)) * 16;
      u32x4 av[4], mv[4];
      for (u32 spin = 0; spin < SPIN_CAP; ++spin) {
        asm volatile("" ::: "memory");
        bool ok = true;
#pragma unroll
        for (int i = 0; i < 4; ++i) { av[i] = GET16(sa + i * 1024); mv[i] = GET16(sm + i * 128); }
#pragma unroll
        for (int i = 0; i < 4; ++i) ok &= (av[i][1] == tag) & (av[i][3] == tag) & (mv[i][1] == tag) & (mv[i][3] == tag);
        if (__all(ok)) break;
      }
      float M = fmaxf(fmaxf(__uint_as_float(mv[0][0]), __uint_as_float(mv[1][0])), fmaxf(__uint_as_float(mv[2][0]), __uint_as_float(mv[3][0])));
      M = max_m8(M);
      float L = 0.f, o0 = 0.f, o1 = 0.f;
#pragma unroll
      for (int i = 0; i < 4; ++i) {
        const float w = __builtin_amdgcn_exp2f((__uint_as_float(mv[i][0]) - M) * LOG2E);
        L = fmaf(w, __uint_as_float(mv[i][2]), L); o0 = fmaf(w, __uint_as_float(av[i][0]), o0); o1 = fmaf(w, __uint_as_float(av[i][2]), o1);
      }
      L = sum_m8(L); o0 = sum_m8(o0); o1 = sum_m8(o1);
      if (lane < 8) *(f32x2*)&sm_ov[2 * lane] = f32x2{bfr(o0 / L), bfr(o1 / L)};
    }
    wg_barrier();
    if (b < NRED) {
      wg_barrier();
      if (lane < 8) {
        float a0 = 0.f, a1 = 0.f, f0 = 0.f, f1 = 0.f;
#pragma unroll
        for (int w = 0; w < NSW; ++w) {
          a0 += sm_fin[64 + w * 16 + 2 * lane]; a1 += sm_fin[64 + w * 16 + 2 * lane + 1];
          f0 += sm_fin[w * 16 + 2 * lane]; f1 += sm_fin[w * 16 + 2 * lane + 1];
        }
        const float o0 = bfr(bflo(xo) + bfr(a0)) + bfr(f0), o1 = bfr(bfhi(xo) + bfr(a1)) + bfr(f1);
        out[8 * b + lane] = cvtpk(o0, o1);
        if (b == 0 && lane == 0) __hip_atomic_store((gu32*)(ws + WS_EPOCH), tag, RLX_AGENT);
      }
    }
  }
}
}

extern "C" void kernel_launch(void* const* d_in, const int* in_sizes, int n_in,
                              void* d_out, int out_size, void* d_ws, size_t ws_size,
                              hipStream_t stream) {
  (void)in_sizes; (void)n_in; (void)out_size; (void)ws_size;
  hipLaunchKernelGGL(fused_decode, dim3(NWG), dim3(NTHR), 0, stream,
                     (const u16*)d_in[0], (const char*)d_in[1], (const char*)d_in[2], (const u16*)d_in[3], (const u16*)d_in[4],
                     (const u16*)d_in[6], (const u16*)d_in[7], (const u16*)d_in[8], (u32*)d_out, (char*)d_ws);
}
```
